```python
import torch

def _softmax(x, axis=-1):
    return torch.softmax(x, dim=axis)
def _gelu(x):
    return torch.nn.functional.gelu(x, approximate="tanh")
def _silu(x):
    return torch.nn.functional.silu(x)
def _sigmoid(x):
    return torch.sigmoid(x)
def _layernorm(x, axis=-1, eps=1e-5):
    m = x.mean(dim=axis, keepdim=True)
    v = x.var(dim=axis, keepdim=True, unbiased=False)
    return (x - m) / torch.sqrt(v + eps)
def _scatter_add(tgt, idx, src, axis=0):
    return tgt.clone().index_add_(axis, idx, src)
def _segment_sum(src, seg, n):
    out = torch.zeros((n,) + src.shape[1:], dtype=src.dtype)
    return out.index_add_(0, seg, src)
def _pad(x, pw):
    flat = tuple(v for pair in reversed(pw) for v in pair)
    return torch.nn.functional.pad(x, flat)

kernel_name = 'parallel_sum__ssm_conv_h3__glu_gated_glu__prompt'

def setup_inputs():

    _w = (lambda a: (lambda t: t.to(torch.bfloat16) if t.is_floating_point() else t)(torch.as_tensor(a)))
    def _A_setup():
        B, S, D = 1, 2048, 640
        ks = 4
        return {
            "q": _w((lambda *s: torch.randn(*s, dtype=torch.bfloat16) * 0.05)(B, S, D)),
            "k": _w((lambda *s: torch.randn(*s, dtype=torch.bfloat16) * 0.4)(B, S, D)),
            "v": _w((lambda *s: torch.randn(*s, dtype=torch.bfloat16) * 0.4)(B, S, D)),
            "shift_k": _w((lambda *s: torch.randn(*s, dtype=torch.bfloat16) * 0.4)(D, ks) * 0.5),
            "ssm_k": _w((lambda *s: torch.randn(*s, dtype=torch.bfloat16) * 1.6)(D, S) * (1.0 / S ** 0.5)),
            "ssm_D": _w((lambda *s: torch.randn(*s, dtype=torch.bfloat16) * 0.4)(D)),
        }
    _A_in = _A_setup()

    def _B_setup():
        B, S, D, F = 1, 2048, 640, 1728
        return {
            "x": _w((lambda *s: torch.randn(*s, dtype=torch.bfloat16) * 0.05)(B, S, D)),
            "w_gate": _w((lambda *s: torch.randn(*s, dtype=torch.bfloat16) * 0.05)(D, F)),
            "w_up": _w((lambda *s: torch.randn(*s, dtype=torch.bfloat16) * 0.05)(D, F)),
            "w_down": _w((lambda *s: torch.randn(*s, dtype=torch.bfloat16) * 0.05)(F, D)),
        }
    _B_in = _B_setup()

    return {
        "A_q": _A_in["q"],
        "A_k": _A_in["k"],
        "A_v": _A_in["v"],
        "A_shift_k": _A_in["shift_k"],
        "A_ssm_k": _A_in["ssm_k"],
        "A_ssm_D": _A_in["ssm_D"],
        "B_x": _B_in["x"],
        "B_w_gate": _B_in["w_gate"],
        "B_w_up": _B_in["w_up"],
        "B_w_down": _B_in["w_down"],
    }

def reference(A_q, A_k, A_v, A_shift_k, A_ssm_k, A_ssm_D, B_x, B_w_gate, B_w_up, B_w_down):
    def _A_ref(q, k, v, shift_k, ssm_k, ssm_D):
        B, S, D = q.shape
        ks = shift_k.shape[-1]
        k_pad = _pad(k, ((0, 0), (ks - 1, 0), (0, 0)))
        k_shift = torch.zeros_like(k)
        for t in range(ks):
            k_shift = k_shift + k_pad[:, t:t + S, :] * shift_k[:, ks - 1 - t][None, None, :]
        kv = k_shift * v
        L = 2 * S
        kv_pad = _pad(kv, ((0, 0), (0, S), (0, 0)))
        kf_pad = _pad(ssm_k, ((0, 0), (0, S)))
        kv_f = (lambda x, axes=(-2,-1): torch.fft.rfft2(x.float(), dim=axes))(kv_pad[:, None, :, :], axes=(1, 2))
        kf_f = (lambda x, axes=(-2,-1): torch.fft.rfft2(x.float(), dim=axes))(kf_pad[:, None, :], axes=(1, 2))
        kf_f = (lambda x, *d: x.permute(*d))(kf_f, 1, 2, 0)
        y_f = kv_f * kf_f[None, :, :, :]
        y = (lambda x, s, axes=(-2,-1): torch.fft.irfft2(x, s=s, dim=axes).to(torch.bfloat16))(y_f, (1, L), axes=(1, 2))[:, 0, :S, :]
        y = y + kv * ssm_D[None, None, :]
        return q * y

    def _B_ref(x, w_gate, w_up, w_down):
        g = torch.einsum("bsd,df->bsf", x, w_gate)
        u = torch.einsum("bsd,df->bsf", x, w_up)
        h = _sigmoid(g) * u
        return torch.einsum("bsf,fd->bsd", h, w_down)

    x = A_q
    xn = _layernorm(x, axis=-1)
    return x + _A_ref(xn, A_k, A_v, A_shift_k, A_ssm_k, A_ssm_D) + _B_ref(xn, B_w_gate, B_w_up, B_w_down)
```

```cpp
#include <hip/hip_runtime.h>
#include <hip/hip_bf16.h>
#include <stdint.h>


typedef unsigned short u16;
using bf16x8 = __attribute__((ext_vector_type(8))) short;
using f32x4 = __attribute__((ext_vector_type(4))) float;
using f32x16 = __attribute__((ext_vector_type(16))) float;

constexpr int S_ = 2048, D_ = 640, F_ = 1728;

__device__ __forceinline__ float bf2f(u16 v) { return __uint_as_float(((unsigned)v) << 16); }
__device__ __forceinline__ u16 f2bf(float f) {
  __hip_bfloat16 b = __float2bfloat16(f);
  return *reinterpret_cast<u16*>(&b);
}
__device__ __forceinline__ float rbf(float f) { return bf2f(f2bf(f)); }

constexpr int PREP_LN_BLOCKS = S_ / 4;
constexpr int PREP_KV_BLOCKS = (S_ / 64) * (D_ / 64);
constexpr int PREP_W1_BLOCKS = (D_ / 64) * (F_ / 64) * 2;
constexpr int PREP_W2_BLOCKS = (F_ / 64) * (D_ / 64);
constexpr int PREP_BLOCKS = PREP_LN_BLOCKS + PREP_KV_BLOCKS + PREP_W1_BLOCKS + PREP_W2_BLOCKS;

__global__ __launch_bounds__(256) void prep_kernel(
    const u16* __restrict__ x, const u16* __restrict__ kin, const u16* __restrict__ vin,
    const u16* __restrict__ shiftk, const u16* __restrict__ wg, const u16* __restrict__ wu,
    const u16* __restrict__ wd, u16* __restrict__ xn, u16* __restrict__ uT,
    u16* __restrict__ w1t, u16* __restrict__ w2t) {
  __shared__ u16 tile[64][66];
  int b = blockIdx.x;
  int tid = threadIdx.x;
  if (b < PREP_LN_BLOCKS) {
    int row = b * 4 + (tid >> 6);
    int lane = tid & 63;
    const unsigned* xr = reinterpret_cast<const unsigned*>(x + (size_t)row * D_);
    float v[10];
#pragma unroll
    for (int i = 0; i < 5; ++i) {
      unsigned p = xr[lane + 64 * i];
      v[2 * i] = bf2f((u16)(p & 0xffff));
      v[2 * i + 1] = bf2f((u16)(p >> 16));
    }
    float s = 0.f;
#pragma unroll
    for (int i = 0; i < 10; ++i) s += v[i];
#pragma unroll
    for (int o = 32; o > 0; o >>= 1) s += __shfl_xor(s, o);
    float mean = s * (1.0f / D_);
    float q = 0.f;
#pragma unroll
    for (int i = 0; i < 10; ++i) { float d = v[i] - mean; q += d * d; }
#pragma unroll
    for (int o = 32; o > 0; o >>= 1) q += __shfl_xor(q, o);
    float var = q * (1.0f / D_);
    float mb = rbf(mean);
    float vb = rbf(var);
    float den = rbf(sqrtf(rbf(vb + 1e-5f)));
    unsigned* xo = reinterpret_cast<unsigned*>(xn + (size_t)row * D_);
#pragma unroll
    for (int i = 0; i < 5; ++i) {
      u16 a = f2bf(rbf(v[2 * i] - mb) / den);
      u16 c = f2bf(rbf(v[2 * i + 1] - mb) / den);
      xo[lane + 64 * i] = (unsigned)a | ((unsigned)c << 16);
    }
    return;
  }
  b -= PREP_LN_BLOCKS;
  if (b < PREP_KV_BLOCKS) {
    int st = (b / (D_ / 64)) * 64, ct = (b % (D_ / 64)) * 64;
    int c = ct + (tid & 63);
    float w0 = bf2f(shiftk[c * 4 + 0]), w1 = bf2f(shiftk[c * 4 + 1]);
    float w2 = bf2f(shiftk[c * 4 + 2]), w3 = bf2f(shiftk[c * 4 + 3]);
    int r0 = (tid >> 6) * 16;
    int s0 = st + r0;
    float km3 = s0 >= 3 ? bf2f(kin[(size_t)(s0 - 3) * D_ + c]) : 0.f;
    float km2 = s0 >= 2 ? bf2f(kin[(size_t)(s0 - 2) * D_ + c]) : 0.f;
    float km1 = s0 >= 1 ? bf2f(kin[(size_t)(s0 - 1) * D_ + c]) : 0.f;
#pragma unroll
    for (int r = 0; r < 16; ++r) {
      int s = s0 + r;
      float k0 = bf2f(kin[(size_t)s * D_ + c]);
      float acc = rbf(km3 * w3);
      acc = rbf(acc + rbf(km2 * w2));
      acc = rbf(acc + rbf(km1 * w1));
      acc = rbf(acc + rbf(k0 * w0));
      float kv = acc * bf2f(vin[(size_t)s * D_ + c]);
      tile[tid & 63][r0 + r] = f2bf(kv);
      km3 = km2; km2 = km1; km1 = k0;
    }
    __syncthreads();
    for (int i = tid; i < 64 * 32; i += 256) {
      int cc = i >> 5, p = (i & 31) * 2;
      int g = p & ~7, e = p & 7;
      u16 a = tile[cc][g + 7 - e], c2 = tile[cc][g + 7 - (e + 1)];
      *reinterpret_cast<unsigned*>(uT + (size_t)(ct + cc) * S_ + st + p) = (unsigned)a | ((unsigned)c2 << 16);
    }
    return;
  }
  b -= PREP_KV_BLOCKS;
  if (b < PREP_W1_BLOCKS) {
    int which = b / (PREP_W1_BLOCKS / 2);
    b %= (PREP_W1_BLOCKS / 2);
    const u16* w = which ? wu : wg;
    int kt = (b / (F_ / 64)) * 64, ft = (b % (F_ / 64)) * 64;
    for (int i = tid; i < 64 * 32; i += 256) {
      int r = i >> 5, c = (i & 31) * 2;
      unsigned p = *reinterpret_cast<const unsigned*>(w + (size_t)(kt + r) * F_ + ft + c);
      tile[r][c] = (u16)(p & 0xffff);
      tile[r][c + 1] = (u16)(p >> 16);
    }
    __syncthreads();
    for (int i = tid; i < 64 * 32; i += 256) {
      int f = i >> 5, k = (i & 31) * 2;
      int ff = ft + f;
      int prow = (ff >> 5) * 64 + which * 32 + (ff & 31);
      unsigned p = (unsigned)tile[k][f] | ((unsigned)tile[k + 1][f] << 16);
      *reinterpret_cast<unsigned*>(w1t + (size_t)prow * D_ + kt + k) = p;
    }
    return;
  }
  b -= PREP_W1_BLOCKS;
  {
    int kt = (b / (D_ / 64)) * 64, nt = (b % (D_ / 64)) * 64;
    for (int i = tid; i < 64 * 32; i += 256) {
      int r = i >> 5, c = (i & 31) * 2;
      unsigned p = *reinterpret_cast<const unsigned*>(wd + (size_t)(kt + r) * D_ + nt + c);
      tile[r][c] = (u16)(p & 0xffff);
      tile[r][c + 1] = (u16)(p >> 16);
    }
    __syncthreads();
    for (int i = tid; i < 64 * 32; i += 256) {
      int n = i >> 5, k = (i & 31) * 2;
      unsigned p = (unsigned)tile[k][n] | ((unsigned)tile[k + 1][n] << 16);
      *reinterpret_cast<unsigned*>(w2t + (size_t)(nt + n) * F_ + kt + k) = p;
    }
  }
}

template <int BM, int BN>
__device__ __forceinline__ void gemm_mainloop(const u16* __restrict__ A, const u16* __restrict__ B,
                                              int K, int brow, int bcol, char* lds,
                                              f32x4 (&acc)[BM / 32][BN / 32]) {
  constexpr int ABYTES = BM * 128, BBYTES = BN * 128, STAGE = ABYTES + BBYTES;
  const int tid = threadIdx.x, wid = tid >> 6, lane = tid & 63;
  const int wr = wid >> 1, wc = wid & 1, fr = lane & 15, fq = lane >> 4;
  auto stage = [&](int buf, int kt) {
    char* base = lds + buf * STAGE;
#pragma unroll
    for (int i = 0; i < BM / 32; ++i) {
      int bb = tid * 16 + i * 4096;
      int r = bb >> 7, pc = (bb >> 4) & 7;
      int sc = pc ^ (r & 7);
      __builtin_amdgcn_global_load_lds(
          (const __attribute__((address_space(1))) void*)(A + (size_t)(brow + r) * K + kt * 64 + sc * 8),
          (__attribute__((address_space(3))) void*)(base + bb), 16, 0, 0);
    }
#pragma unroll
    for (int i = 0; i < BN / 32; ++i) {
      int bb = tid * 16 + i * 4096;
      int r = bb >> 7, pc = (bb >> 4) & 7;
      int sc = pc ^ (r & 7);
      __builtin_amdgcn_global_load_lds(
          (const __attribute__((address_space(1))) void*)(B + (size_t)(bcol + r) * K + kt * 64 + sc * 8),
          (__attribute__((address_space(3))) void*)(base + ABYTES + bb), 16, 0, 0);
    }
  };
  const int nt = K / 64;
  stage(0, 0);
  asm volatile("s_waitcnt vmcnt(0)" ::: "memory");
  __syncthreads();
  for (int t = 0; t < nt; ++t) {
    int cur = t & 1;
    if (t + 1 < nt) stage(cur ^ 1, t + 1);
    char* sa = lds + cur * STAGE;
    char* sb = sa + ABYTES;
#pragma unroll
    for (int kk = 0; kk < 2; ++kk) {
      bf16x8 af[BM / 32], bfr[BN / 32];
#pragma unroll
      for (int m = 0; m < BM / 32; ++m) {
        int r = wr * (BM / 2) + m * 16 + fr;
        int c = (kk * 4 + fq) ^ (r & 7);
        af[m] = *reinterpret_cast<const bf16x8*>(sa + r * 128 + c * 16);
      }
#pragma unroll
      for (int n = 0; n < BN / 32; ++n) {
        int r = wc * (BN / 2) + n * 16 + fr;
        int c = (kk * 4 + fq) ^ (r & 7);
        bfr[n] = *reinterpret_cast<const bf16x8*>(sb + r * 128 + c * 16);
      }
#pragma unroll
      for (int m = 0; m < BM / 32; ++m)
#pragma unroll
        for (int n = 0; n < BN / 32; ++n)
          acc[m][n] = __builtin_amdgcn_mfma_f32_16x16x32_bf16(af[m], bfr[n], acc[m][n], 0, 0, 0);
    }
    asm volatile("s_waitcnt vmcnt(0)" ::: "memory");
    __syncthreads();
  }
}

constexpr int G1_BM = 128, G1_BN = 128;
constexpr int G1_BLOCKS = (S_ / G1_BM) * (2 * F_ / G1_BN);
constexpr int CONV_BLOCKS = D_ / 4;
constexpr int HC_STRIDE = 4384;
constexpr int CONV_WAVE_LDS = 8 * HC_STRIDE + S_ * 2;
constexpr int L2_LDS = 4 * CONV_WAVE_LDS;

__global__ __launch_bounds__(256) void mid_kernel(
    const u16* __restrict__ xn, const u16* __restrict__ w1t, u16* __restrict__ hbuf,
    const u16* __restrict__ uT, const u16* __restrict__ ssmk, const u16* __restrict__ ssmd,
    u16* __restrict__ yT) {
  extern __shared__ __attribute__((aligned(16))) char lds[];
  const int tid = threadIdx.x, wid = tid >> 6, lane = tid & 63;
  int b = blockIdx.x;
  if (b < G1_BLOCKS) {
    int bm = b % (S_ / G1_BM), bn = b / (S_ / G1_BM);
    int brow = bm * G1_BM, bcol = bn * G1_BN;
    f32x4 acc[4][4];
#pragma unroll
    for (int m = 0; m < 4; ++m)
#pragma unroll
      for (int n = 0; n < 4; ++n) acc[m][n] = f32x4{0.f, 0.f, 0.f, 0.f};
    gemm_mainloop<G1_BM, G1_BN>(xn, w1t, D_, brow, bcol, lds, acc);
    const int wr = wid >> 1, wc = wid & 1, fr = lane & 15, fq = lane >> 4;
    int f0 = bn * 64 + wc * 32;
#pragma unroll
    for (int m = 0; m < 4; ++m)
#pragma unroll
      for (int n = 0; n < 2; ++n)
#pragma unroll
        for (int j = 0; j < 4; ++j) {
          float g = rbf(acc[m][n][j]);
          float u = rbf(acc[m][n + 2][j]);
          float sg = rbf(1.0f / (1.0f + __expf(-g)));
          int row = brow + wr * 64 + m * 16 + fq * 4 + j;
          hbuf[(size_t)row * F_ + f0 + n * 16 + fr] = f2bf(sg * u);
        }
    return;
  }
  b -= G1_BLOCKS;
  const int c = b * 4 + wid;
  char* wl = lds + wid * CONV_WAVE_LDS;
  u16* ul = reinterpret_cast<u16*>(wl + 8 * HC_STRIDE);
  const u16* hrow = ssmk + (size_t)c * S_;
  for (int y0 = lane * 8; y0 < S_ + 32; y0 += 64 * 8) {
    u16 e[16];
#pragma unroll
    for (int i = 0; i < 16; ++i) {
      int z = y0 + i - 32;
      e[i] = (z >= 0 && z < S_) ? hrow[z] : (u16)0;
    }
#pragma unroll
    for (int a = 0; a < 8; ++a) {
      bf16x8 v;
#pragma unroll
      for (int i = 0; i < 8; ++i) v[i] = (short)e[a + i];
      *reinterpret_cast<bf16x8*>(wl + a * HC_STRIDE + y0 * 2) = v;
    }
  }
  for (int i = lane * 8; i < S_; i += 64 * 8)
    *reinterpret_cast<bf16x8*>(ul + i) = *reinterpret_cast<const bf16x8*>(uT + (size_t)c * S_ + i);
  __syncthreads();
  const int j = lane & 31, hh = lane >> 5;
  f32x16 acc0, acc1;
#pragma unroll
  for (int i = 0; i < 16; ++i) { acc0[i] = 0.f; acc1[i] = 0.f; }
  const int boff = j - 8 * hh - 7 + 32;
  for (int d = 0; d < 64; ++d) {
#pragma unroll
    for (int ks = 0; ks < 2; ++ks) {
      int bp = 32 * d - 16 * ks + boff;
      int a = bp & 7;
      bf16x8 af = *reinterpret_cast<const bf16x8*>(wl + a * HC_STRIDE + (bp - a) * 2);
      {
        int blk = 32 + j - d;
        bool ok = blk >= 0;
        bf16x8 bf = *reinterpret_cast<const bf16x8*>(ul + (ok ? blk : 0) * 32 + 16 * ks + 8 * hh);
        if (!ok) bf = bf16x8{0, 0, 0, 0, 0, 0, 0, 0};
        acc1 = __builtin_amdgcn_mfma_f32_32x32x16_bf16(af, bf, acc1, 0, 0, 0);
      }
      if (d < 32) {
        int blk = j - d;
        bool ok = blk >= 0;
        bf16x8 bf = *reinterpret_cast<const bf16x8*>(ul + (ok ? blk : 0) * 32 + 16 * ks + 8 * hh);
        if (!ok) bf = bf16x8{0, 0, 0, 0, 0, 0, 0, 0};
        acc0 = __builtin_amdgcn_mfma_f32_32x32x16_bf16(af, bf, acc0, 0, 0, 0);
      }
    }
  }
  float dd = bf2f(ssmd[c]);
#pragma unroll
  for (int nt = 0; nt < 2; ++nt) {
#pragma unroll
    for (int r = 0; r < 16; ++r) {
      int row = (r & 3) + 8 * (r >> 2) + 4 * hh;
      int t = 32 * (32 * nt + j) + row;
      float a = nt ? acc1[r] : acc0[r];
      float kv = bf2f(ul[(t & ~7) + 7 - (t & 7)]);
      float y = rbf(rbf(a) + rbf(kv * dd));
      yT[(size_t)c * S_ + t] = f2bf(y);
    }
  }
}

constexpr int G2_BM = 128, G2_BN = 64;
constexpr int G2_BLOCKS = (S_ / G2_BM) * (D_ / G2_BN);

__global__ __launch_bounds__(256) void out_kernel(
    const u16* __restrict__ hbuf, const u16* __restrict__ w2t, const u16* __restrict__ x,
    const u16* __restrict__ xn, const u16* __restrict__ yT, u16* __restrict__ out) {
  extern __shared__ __attribute__((aligned(16))) char lds[];
  const int tid = threadIdx.x, wid = tid >> 6, lane = tid & 63;
  int b = blockIdx.x;
  int bm = b % (S_ / G2_BM), bn = b / (S_ / G2_BM);
  int brow = bm * G2_BM, bcol = bn * G2_BN;
  f32x4 acc[4][2];
#pragma unroll
  for (int m = 0; m < 4; ++m)
#pragma unroll
    for (int n = 0; n < 2; ++n) acc[m][n] = f32x4{0.f, 0.f, 0.f, 0.f};
  gemm_mainloop<G2_BM, G2_BN>(hbuf, w2t, F_, brow, bcol, lds, acc);
  const int wr = wid >> 1, wc = wid & 1, fr = lane & 15, fq = lane >> 4;
#pragma unroll
  for (int m = 0; m < 4; ++m)
#pragma unroll
    for (int n = 0; n < 2; ++n)
#pragma unroll
      for (int j = 0; j < 4; ++j) {
        int row = brow + wr * 64 + m * 16 + fq * 4 + j;
        int col = bcol + wc * 32 + n * 16 + fr;
        size_t idx = (size_t)row * D_ + col;
        float a = rbf(bf2f(xn[idx]) * bf2f(yT[(size_t)col * S_ + row]));
        float o = rbf(bf2f(x[idx]) + a);
        out[idx] = f2bf(o + rbf(acc[m][n][j]));
      }
}

extern "C" void kernel_launch(void* const* d_in, const int* in_sizes, int n_in,
                              void* d_out, int out_size, void* d_ws, size_t ws_size,
                              hipStream_t stream) {
  const u16* x = (const u16*)d_in[0];
  const u16* kin = (const u16*)d_in[1];
  const u16* vin = (const u16*)d_in[2];
  const u16* shiftk = (const u16*)d_in[3];
  const u16* ssmk = (const u16*)d_in[4];
  const u16* ssmd = (const u16*)d_in[5];
  const u16* wg = (const u16*)d_in[7];
  const u16* wu = (const u16*)d_in[8];
  const u16* wd = (const u16*)d_in[9];
  u16* out = (u16*)d_out;
  char* ws = (char*)d_ws;
  u16* xn = (u16*)ws;                       ws += (size_t)S_ * D_ * 2;
  u16* uT = (u16*)ws;                       ws += (size_t)S_ * D_ * 2;
  u16* yT = (u16*)ws;                       ws += (size_t)S_ * D_ * 2;
  u16* w1t = (u16*)ws;                      ws += (size_t)2 * F_ * D_ * 2;
  u16* w2t = (u16*)ws;                      ws += (size_t)F_ * D_ * 2;
  u16* hbuf = (u16*)ws;                     ws += (size_t)S_ * F_ * 2;

  hipFuncSetAttribute((const void*)mid_kernel, hipFuncAttributeMaxDynamicSharedMemorySize, L2_LDS);
  prep_kernel<<<PREP_BLOCKS, 256, 0, stream>>>(x, kin, vin, shiftk, wg, wu, wd, xn, uT, w1t, w2t);
  mid_kernel<<<G1_BLOCKS + CONV_BLOCKS, 256, L2_LDS, stream>>>(xn, w1t, hbuf, uT, ssmk, ssmd, yT);
  out_kernel<<<G2_BLOCKS, 256, 2 * (G2_BM + G2_BN) * 128, stream>>>(hbuf, w2t, x, xn, yT, out);
}
```

```cpp
#include <hip/hip_runtime.h>
#include <hip/hip_bf16.h>
#include <stdint.h>


typedef unsigned short u16;
using bf16x8 = __attribute__((ext_vector_type(8))) short;
using f32x4 = __attribute__((ext_vector_type(4))) float;
using f32x16 = __attribute__((ext_vector_type(16))) float;

constexpr int S_ = 2048, D_ = 640, F_ = 1728;

__device__ __forceinline__ float bf2f(u16 v) { return __uint_as_float(((unsigned)v) << 16); }
__device__ __forceinline__ u16 f2bf(float f) {
  __hip_bfloat16 b = __float2bfloat16(f);
  return *reinterpret_cast<u16*>(&b);
}
__device__ __forceinline__ float rbf(float f) { return bf2f(f2bf(f)); }

constexpr int PREP_LN_BLOCKS = S_ / 4;
constexpr int PREP_KV_BLOCKS = (S_ / 64) * (D_ / 64);
constexpr int PREP_W1_BLOCKS = (D_ / 64) * (F_ / 64) * 2;
constexpr int PREP_W2_BLOCKS = (F_ / 64) * (D_ / 64);
constexpr int PREP_BLOCKS = PREP_LN_BLOCKS + PREP_KV_BLOCKS + PREP_W1_BLOCKS + PREP_W2_BLOCKS;

__global__ __launch_bounds__(256) void prep_kernel(
    const u16* __restrict__ x, const u16* __restrict__ kin, const u16* __restrict__ vin,
    const u16* __restrict__ shiftk, const u16* __restrict__ wg, const u16* __restrict__ wu,
    const u16* __restrict__ wd, u16* __restrict__ xn, u16* __restrict__ uT,
    u16* __restrict__ w1t, u16* __restrict__ w2t) {
  __shared__ u16 tile[64][66];
  int b = blockIdx.x;
  int tid = threadIdx.x;
  if (b < PREP_LN_BLOCKS) {
    int row = b * 4 + (tid >> 6);
    int lane = tid & 63;
    const unsigned* xr = reinterpret_cast<const unsigned*>(x + (size_t)row * D_);
    float v[10];
#pragma unroll
    for (int i = 0; i < 5; ++i) {
      unsigned p = xr[lane + 64 * i];
      v[2 * i] = bf2f((u16)(p & 0xffff));
      v[2 * i + 1] = bf2f((u16)(p >> 16));
    }
    float s = 0.f;
#pragma unroll
    for (int i = 0; i < 10; ++i) s += v[i];
#pragma unroll
    for (int o = 32; o > 0; o >>= 1) s += __shfl_xor(s, o);
    float mean = s * (1.0f / D_);
    float q = 0.f;
#pragma unroll
    for (int i = 0; i < 10; ++i) { float d = v[i] - mean; q += d * d; }
#pragma unroll
    for (int o = 32; o > 0; o >>= 1) q += __shfl_xor(q, o);
    float var = q * (1.0f / D_);
    float mb = rbf(mean);
    float vb = rbf(var);
    float den = rbf(sqrtf(rbf(vb + 1e-5f)));
    unsigned* xo = reinterpret_cast<unsigned*>(xn + (size_t)row * D_);
#pragma unroll
    for (int i = 0; i < 5; ++i) {
      u16 a = f2bf(rbf(v[2 * i] - mb) / den);
      u16 c = f2bf(rbf(v[2 * i + 1] - mb) / den);
      xo[lane + 64 * i] = (unsigned)a | ((unsigned)c << 16);
    }
    return;
  }
  b -= PREP_LN_BLOCKS;
  if (b < PREP_KV_BLOCKS) {
    int st = (b / (D_ / 64)) * 64, ct = (b % (D_ / 64)) * 64;
    int c = ct + (tid & 63);
    float w0 = bf2f(shiftk[c * 4 + 0]), w1 = bf2f(shiftk[c * 4 + 1]);
    float w2 = bf2f(shiftk[c * 4 + 2]), w3 = bf2f(shiftk[c * 4 + 3]);
    int r0 = (tid >> 6) * 16;
    int s0 = st + r0;
    float km3 = s0 >= 3 ? bf2f(kin[(size_t)(s0 - 3) * D_ + c]) : 0.f;
    float km2 = s0 >= 2 ? bf2f(kin[(size_t)(s0 - 2) * D_ + c]) : 0.f;
    float km1 = s0 >= 1 ? bf2f(kin[(size_t)(s0 - 1) * D_ + c]) : 0.f;
#pragma unroll
    for (int r = 0; r < 16; ++r) {
      int s = s0 + r;
      float k0 = bf2f(kin[(size_t)s * D_ + c]);
      float acc = rbf(km3 * w3);
      acc = rbf(acc + rbf(km2 * w2));
      acc = rbf(acc + rbf(km1 * w1));
      acc = rbf(acc + rbf(k0 * w0));
      float kv = acc * bf2f(vin[(size_t)s * D_ + c]);
      tile[tid & 63][r0 + r] = f2bf(kv);
      km3 = km2; km2 = km1; km1 = k0;
    }
    __syncthreads();
    for (int i = tid; i < 64 * 32; i += 256) {
      int cc = i >> 5, p = (i & 31) * 2;
      int g = p & ~7, e = p & 7;
      u16 a = tile[cc][g + 7 - e], c2 = tile[cc][g + 7 - (e + 1)];
      *reinterpret_cast<unsigned*>(uT + (size_t)(ct + cc) * S_ + st + p) = (unsigned)a | ((unsigned)c2 << 16);
    }
    return;
  }
  b -= PREP_KV_BLOCKS;
  if (b < PREP_W1_BLOCKS) {
    int which = b / (PREP_W1_BLOCKS / 2);
    b %= (PREP_W1_BLOCKS / 2);
    const u16* w = which ? wu : wg;
    int kt = (b / (F_ / 64)) * 64, ft = (b % (F_ / 64)) * 64;
    for (int i = tid; i < 64 * 32; i += 256) {
      int r = i >> 5, c = (i & 31) * 2;
      unsigned p = *reinterpret_cast<const unsigned*>(w + (size_t)(kt + r) * F_ + ft + c);
      tile[r][c] = (u16)(p & 0xffff);
      tile[r][c + 1] = (u16)(p >> 16);
    }
    __syncthreads();
    for (int i = tid; i < 64 * 32; i += 256) {
      int f = i >> 5, k = (i & 31) * 2;
      int ff = ft + f;
      int prow = (ff >> 5) * 64 + which * 32 + (ff & 31);
      unsigned p = (unsigned)tile[k][f] | ((unsigned)tile[k + 1][f] << 16);
      *reinterpret_cast<unsigned*>(w1t + (size_t)prow * D_ + kt + k) = p;
    }
    return;
  }
  b -= PREP_W1_BLOCKS;
  {
    int kt = (b / (D_ / 64)) * 64, nt = (b % (D_ / 64)) * 64;
    for (int i = tid; i < 64 * 32; i += 256) {
      int r = i >> 5, c = (i & 31) * 2;
      unsigned p = *reinterpret_cast<const unsigned*>(wd + (size_t)(kt + r) * D_ + nt + c);
      tile[r][c] = (u16)(p & 0xffff);
      tile[r][c + 1] = (u16)(p >> 16);
    }
    __syncthreads();
    for (int i = tid; i < 64 * 32; i += 256) {
      int n = i >> 5, k = (i & 31) * 2;
      unsigned p = (unsigned)tile[k][n] | ((unsigned)tile[k + 1][n] << 16);
      *reinterpret_cast<unsigned*>(w2t + (size_t)(nt + n) * F_ + kt + k) = p;
    }
  }
}

template <int N> __device__ __forceinline__ void wait_vm() {
  asm volatile("s_waitcnt vmcnt(%0)" ::"n"(N) : "memory");
}

template <int BM, int BN, int NS>
__device__ __forceinline__ void gemm_mainloop(const u16* __restrict__ A, const u16* __restrict__ B,
                                              int K, int brow, int bcol, char* lds,
                                              f32x4 (&acc)[BM / 32][BN / 32]) {
  constexpr int ABYTES = BM * 128, BBYTES = BN * 128, STAGE = ABYTES + BBYTES;
  constexpr int L = (BM + BN) / 32;
  const int tid = threadIdx.x, wid = tid >> 6, lane = tid & 63;
  const int wr = wid >> 1, wc = wid & 1, fr = lane & 15, fq = lane >> 4;
  const int sr = tid >> 3, ssc = ((tid & 7) ^ (sr & 7)) * 8;
  const u16* ga = A + (size_t)(brow + sr) * K + ssc;
  const u16* gb = B + (size_t)(bcol + sr) * K + ssc;
  auto stage = [&](int buf, int kt) {
    char* base = lds + buf * STAGE + tid * 16;
#pragma unroll
    for (int i = 0; i < BM / 32; ++i)
      __builtin_amdgcn_global_load_lds(
          (const __attribute__((address_space(1))) void*)(ga + (size_t)i * 32 * K + kt * 64),
          (__attribute__((address_space(3))) void*)(base + i * 4096), 16, 0, 0);
#pragma unroll
    for (int i = 0; i < BN / 32; ++i)
      __builtin_amdgcn_global_load_lds(
          (const __attribute__((address_space(1))) void*)(gb + (size_t)i * 32 * K + kt * 64),
          (__attribute__((address_space(3))) void*)(base + ABYTES + i * 4096), 16, 0, 0);
  };
  const int nt = K / 64;
#pragma unroll
  for (int s = 0; s < NS - 1; ++s) stage(s, s);
  int cur = 0, nxt = NS - 1;
  for (int t = 0; t < nt; ++t) {
    if (t + NS - 1 <= nt) wait_vm<L*(NS - 2)>(); else wait_vm<0>();
    __builtin_amdgcn_s_barrier();
    if (t + NS - 1 < nt) stage(nxt, t + NS - 1);
    char* sa = lds + cur * STAGE;
    char* sb = sa + ABYTES;
#pragma unroll
    for (int kk = 0; kk < 2; ++kk) {
      bf16x8 af[BM / 32], bfr[BN / 32];
#pragma unroll
      for (int m = 0; m < BM / 32; ++m) {
        int r = wr * (BM / 2) + m * 16 + fr;
        int c = (kk * 4 + fq) ^ (r & 7);
        af[m] = *reinterpret_cast<const bf16x8*>(sa + r * 128 + c * 16);
      }
#pragma unroll
      for (int n = 0; n < BN / 32; ++n) {
        int r = wc * (BN / 2) + n * 16 + fr;
        int c = (kk * 4 + fq) ^ (r & 7);
        bfr[n] = *reinterpret_cast<const bf16x8*>(sb + r * 128 + c * 16);
      }
#pragma unroll
      for (int m = 0; m < BM / 32; ++m)
#pragma unroll
        for (int n = 0; n < BN / 32; ++n)
          acc[m][n] = __builtin_amdgcn_mfma_f32_16x16x32_bf16(af[m], bfr[n], acc[m][n], 0, 0, 0);
    }
    cur = (cur + 1 == NS) ? 0 : cur + 1;
    nxt = (nxt + 1 == NS) ? 0 : nxt + 1;
  }
  __builtin_amdgcn_s_barrier();
}

constexpr int G1_BM = 128, G1_BN = 128, G1_NS = 2;
constexpr int G1_BLOCKS = (S_ / G1_BM) * (2 * F_ / G1_BN);
constexpr int CONV_BLOCKS = D_ / 4;
constexpr int HC_STRIDE = 1184;
constexpr int CONV_WAVE_LDS = 8 * HC_STRIDE + S_ * 2;
constexpr int G1_LDS = G1_NS * (G1_BM + G1_BN) * 128;
constexpr int L2_LDS = (4 * CONV_WAVE_LDS > G1_LDS) ? 4 * CONV_WAVE_LDS : G1_LDS;

__global__ __launch_bounds__(256) void mid_kernel(
    const u16* __restrict__ xn, const u16* __restrict__ w1t, u16* __restrict__ hbuf,
    const u16* __restrict__ uT, const u16* __restrict__ ssmk, const u16* __restrict__ ssmd,
    u16* __restrict__ yT) {
  extern __shared__ __attribute__((aligned(16))) char lds[];
  const int tid = threadIdx.x, wid = tid >> 6, lane = tid & 63;
  int b = blockIdx.x;
  if (b >= CONV_BLOCKS) {
    b -= CONV_BLOCKS;
    int bm = b % (S_ / G1_BM), bn = b / (S_ / G1_BM);
    int brow = bm * G1_BM, bcol = bn * G1_BN;
    f32x4 acc[4][4];
#pragma unroll
    for (int m = 0; m < 4; ++m)
#pragma unroll
      for (int n = 0; n < 4; ++n) acc[m][n] = f32x4{0.f, 0.f, 0.f, 0.f};
    gemm_mainloop<G1_BM, G1_BN, G1_NS>(xn, w1t, D_, brow, bcol, lds, acc);
    const int wr = wid >> 1, wc = wid & 1, fr = lane & 15, fq = lane >> 4;
    char* wl = lds + wid * (64 * 72);
#pragma unroll
    for (int m = 0; m < 4; ++m)
#pragma unroll
      for (int n = 0; n < 2; ++n)
#pragma unroll
        for (int j = 0; j < 4; ++j) {
          float g = rbf(acc[m][n][j]);
          float u = rbf(acc[m][n + 2][j]);
          float sg = rbf(1.0f / (1.0f + __expf(-g)));
          *reinterpret_cast<u16*>(wl + (m * 16 + fq * 4 + j) * 72 + (n * 16 + fr) * 2) = f2bf(sg * u);
        }
    int f0 = bn * 64 + wc * 32;
#pragma unroll
    for (int it = 0; it < 4; ++it) {
      int r = it * 16 + (lane >> 2), ch = lane & 3;
      const unsigned* p = reinterpret_cast<const unsigned*>(wl + r * 72 + ch * 16);
      uint4 v = make_uint4(p[0], p[1], p[2], p[3]);
      *reinterpret_cast<uint4*>(hbuf + (size_t)(brow + wr * 64 + r) * F_ + f0 + ch * 8) = v;
    }
    return;
  }
  const int c = b * 4 + wid;
  char* wl = lds + wid * CONV_WAVE_LDS;
  u16* ul = reinterpret_cast<u16*>(wl + 8 * HC_STRIDE);
  const u16* hrow = ssmk + (size_t)c * S_;
  for (int i = lane * 8; i < S_; i += 64 * 8)
    *reinterpret_cast<bf16x8*>(ul + i) = *reinterpret_cast<const bf16x8*>(uT + (size_t)c * S_ + i);
  const int j = lane & 31, hh = lane >> 5;
  f32x16 acc0, acc1;
#pragma unroll
  for (int i = 0; i < 16; ++i) { acc0[i] = 0.f; acc1[i] = 0.f; }
  const int boff = j - 8 * hh - 7 + 32 + 16;
  for (int d0 = 0; d0 < 64; d0 += 16) {
#pragma unroll
    for (int it = 0; it < 2; ++it) {
      int y0 = (it * 64 + lane) * 8;
      if (y0 < 576) {
        int hi = y0 + 32 * d0 - 48;
        bf16x8 v0 = bf16x8{0, 0, 0, 0, 0, 0, 0, 0}, v1 = v0;
        if (hi >= 0 && hi < S_) v0 = *reinterpret_cast<const bf16x8*>(hrow + hi);
        if (hi + 8 >= 0 && hi + 8 < S_) v1 = *reinterpret_cast<const bf16x8*>(hrow + hi + 8);
#pragma unroll
        for (int a = 0; a < 8; ++a) {
          bf16x8 v;
#pragma unroll
          for (int i = 0; i < 8; ++i) v[i] = (a + i < 8) ? v0[a + i] : v1[a + i - 8];
          *reinterpret_cast<bf16x8*>(wl + a * HC_STRIDE + y0 * 2) = v;
        }
      }
    }
    for (int dl = 0; dl < 16; ++dl) {
      int d = d0 + dl;
#pragma unroll
      for (int ks = 0; ks < 2; ++ks) {
        int bp = 32 * dl - 16 * ks + boff;
        int a = bp & 7;
        bf16x8 af = *reinterpret_cast<const bf16x8*>(wl + a * HC_STRIDE + (bp - a) * 2);
        {
          int blk = 32 + j - d;
          bool ok = blk >= 0;
          bf16x8 bf = *reinterpret_cast<const bf16x8*>(ul + (ok ? blk : 0) * 32 + 16 * ks + 8 * hh);
          if (!ok) bf = bf16x8{0, 0, 0, 0, 0, 0, 0, 0};
          acc1 = __builtin_amdgcn_mfma_f32_32x32x16_bf16(af, bf, acc1, 0, 0, 0);
        }
        if (d0 < 32) {
          int blk = j - d;
          bool ok = blk >= 0;
          bf16x8 bf = *reinterpret_cast<const bf16x8*>(ul + (ok ? blk : 0) * 32 + 16 * ks + 8 * hh);
          if (!ok) bf = bf16x8{0, 0, 0, 0, 0, 0, 0, 0};
          acc0 = __builtin_amdgcn_mfma_f32_32x32x16_bf16(af, bf, acc0, 0, 0, 0);
        }
      }
    }
  }
  float dd = bf2f(ssmd[c]);
#pragma unroll
  for (int nt = 0; nt < 2; ++nt) {
#pragma unroll
    for (int rq = 0; rq < 4; ++rq) {
      int t0 = 32 * (32 * nt + j) + 8 * rq + 4 * hh;
      u16 o[4];
#pragma unroll
      for (int r = 0; r < 4; ++r) {
        int t = t0 + r;
        float a = nt ? acc1[rq * 4 + r] : acc0[rq * 4 + r];
        float kv = bf2f(ul[(t & ~7) + 7 - (t & 7)]);
        o[r] = f2bf(rbf(a) + rbf(kv * dd));
      }
      *reinterpret_cast<uint2*>(yT + (size_t)c * S_ + t0) =
          make_uint2((unsigned)o[0] | ((unsigned)o[1] << 16), (unsigned)o[2] | ((unsigned)o[3] << 16));
    }
  }
}

constexpr int G2_BM = 128, G2_BN = 64, G2_NS = 4;
constexpr int G2_BLOCKS = (S_ / G2_BM) * (D_ / G2_BN);
constexpr int G2_LDS = G2_NS * (G2_BM + G2_BN) * 128;

__global__ __launch_bounds__(256) void out_kernel(
    const u16* __restrict__ hbuf, const u16* __restrict__ w2t, const u16* __restrict__ x,
    const u16* __restrict__ xn, const u16* __restrict__ yT, u16* __restrict__ out) {
  extern __shared__ __attribute__((aligned(16))) char lds[];
  const int tid = threadIdx.x, wid = tid >> 6, lane = tid & 63;
  int b = blockIdx.x;
  int bm = b % (S_ / G2_BM), bn = b / (S_ / G2_BM);
  int brow = bm * G2_BM, bcol = bn * G2_BN;
  f32x4 acc[4][2];
#pragma unroll
  for (int m = 0; m < 4; ++m)
#pragma unroll
    for (int n = 0; n < 2; ++n) acc[m][n] = f32x4{0.f, 0.f, 0.f, 0.f};
  gemm_mainloop<G2_BM, G2_BN, G2_NS>(hbuf, w2t, F_, brow, bcol, lds, acc);
  const int wr = wid >> 1, wc = wid & 1, fr = lane & 15, fq = lane >> 4;
  char* lb = lds;
  char* ly = lds + 128 * 136;
#pragma unroll
  for (int m = 0; m < 4; ++m)
#pragma unroll
    for (int n = 0; n < 2; ++n)
#pragma unroll
      for (int j = 0; j < 4; ++j)
        *reinterpret_cast<u16*>(lb + (wr * 64 + m * 16 + fq * 4 + j) * 136 + (wc * 32 + n * 16 + fr) * 2) =
            f2bf(acc[m][n][j]);
#pragma unroll
  for (int it = 0; it < 4; ++it) {
    int idx = it * 256 + tid;
    int cc = idx >> 4, tq = (idx & 15) * 8;
    bf16x8 v = *reinterpret_cast<const bf16x8*>(yT + (size_t)(bcol + cc) * S_ + brow + tq);
#pragma unroll
    for (int e = 0; e < 8; ++e) *reinterpret_cast<u16*>(ly + (tq + e) * 136 + cc * 2) = (u16)v[e];
  }
  __syncthreads();
#pragma unroll
  for (int it = 0; it < 4; ++it) {
    int idx = it * 256 + tid;
    int r = idx >> 3, ch = idx & 7;
    size_t g = (size_t)(brow + r) * D_ + bcol + ch * 8;
    bf16x8 xv = *reinterpret_cast<const bf16x8*>(x + g);
    bf16x8 nv = *reinterpret_cast<const bf16x8*>(xn + g);
    const u16* pb = reinterpret_cast<const u16*>(lb + r * 136 + ch * 16);
    const u16* py = reinterpret_cast<const u16*>(ly + r * 136 + ch * 16);
    bf16x8 o;
#pragma unroll
    for (int e = 0; e < 8; ++e) {
      float a = rbf(bf2f((u16)nv[e]) * bf2f(py[e]));
      float s1 = rbf(bf2f((u16)xv[e]) + a);
      o[e] = (short)f2bf(s1 + bf2f(pb[e]));
    }
    *reinterpret_cast<bf16x8*>(out + g) = o;
  }
}

extern "C" void kernel_launch(void* const* d_in, const int* in_sizes, int n_in,
                              void* d_out, int out_size, void* d_ws, size_t ws_size,
                              hipStream_t stream) {
  const u16* x = (const u16*)d_in[0];
  const u16* kin = (const u16*)d_in[1];
  const u16* vin = (const u16*)d_in[2];
  const u16* shiftk = (const u16*)d_in[3];
  const u16* ssmk = (const u16*)d_in[4];
  const u16* ssmd = (const u16*)d_in[5];
  const u16* wg = (const u16*)d_in[7];
  const u16* wu = (const u16*)d_in[8];
  const u16* wd = (const u16*)d_in[9];
  u16* out = (u16*)d_out;
  char* ws = (char*)d_ws;
  u16* xn = (u16*)ws;                       ws += (size_t)S_ * D_ * 2;
  u16* uT = (u16*)ws;                       ws += (size_t)S_ * D_ * 2;
  u16* yT = (u16*)ws;                       ws += (size_t)S_ * D_ * 2;
  u16* w1t = (u16*)ws;                      ws += (size_t)2 * F_ * D_ * 2;
  u16* w2t = (u16*)ws;                      ws += (size_t)F_ * D_ * 2;
  u16* hbuf = (u16*)ws;                     ws += (size_t)S_ * F_ * 2;

  hipFuncSetAttribute((const void*)mid_kernel, hipFuncAttributeMaxDynamicSharedMemorySize, L2_LDS);
  prep_kernel<<<PREP_BLOCKS, 256, 0, stream>>>(x, kin, vin, shiftk, wg, wu, wd, xn, uT, w1t, w2t);
  mid_kernel<<<G1_BLOCKS + CONV_BLOCKS, 256, L2_LDS, stream>>>(xn, w1t, hbuf, uT, ssmk, ssmd, yT);
  hipFuncSetAttribute((const void*)out_kernel, hipFuncAttributeMaxDynamicSharedMemorySize, G2_LDS);
  out_kernel<<<G2_BLOCKS, 256, G2_LDS, stream>>>(hbuf, w2t, x, xn, yT, out);
}
```

```cpp
#include <hip/hip_runtime.h>
#include <hip/hip_bf16.h>
#include <stdint.h>

typedef unsigned short u16;
using bf16x8 = __attribute__((ext_vector_type(8))) short;
using f32x4 = __attribute__((ext_vector_type(4))) float;
using f32x16 = __attribute__((ext_vector_type(16))) float;

constexpr int S_ = 2048, D_ = 640, F_ = 1728;

__device__ __forceinline__ float bf2f(u16 v) { return __uint_as_float(((unsigned)v) << 16); }
__device__ __forceinline__ u16 f2bf(float f) {
  __hip_bfloat16 b = __float2bfloat16(f);
  return *reinterpret_cast<u16*>(&b);
}
__device__ __forceinline__ float rbf(float f) { return bf2f(f2bf(f)); }

constexpr int PREP_LN_BLOCKS = S_ / 4;
constexpr int PREP_KV_BLOCKS = (S_ / 64) * (D_ / 64);
constexpr int PREP_W1_BLOCKS = (D_ / 64) * (F_ / 64) * 2;
constexpr int PREP_W2_BLOCKS = (F_ / 64) * (D_ / 64);
constexpr int PREP_BLOCKS = PREP_LN_BLOCKS + PREP_KV_BLOCKS + PREP_W1_BLOCKS + PREP_W2_BLOCKS;

__global__ __launch_bounds__(256) void prep_kernel(
    const u16* __restrict__ x, const u16* __restrict__ kin, const u16* __restrict__ vin,
    const u16* __restrict__ shiftk, const u16* __restrict__ wg, const u16* __restrict__ wu,
    const u16* __restrict__ wd, u16* __restrict__ xn, u16* __restrict__ uT,
    u16* __restrict__ w1t, u16* __restrict__ w2t) {
  __shared__ u16 tile[64][66];
  int b = blockIdx.x;
  int tid = threadIdx.x;
  if (b < PREP_LN_BLOCKS) {
    int row = b * 4 + (tid >> 6);
    int lane = tid & 63;
    const unsigned* xr = reinterpret_cast<const unsigned*>(x + (size_t)row * D_);
    float v[10];
#pragma unroll
    for (int i = 0; i < 5; ++i) {
      unsigned p = xr[lane + 64 * i];
      v[2 * i] = bf2f((u16)(p & 0xffff));
      v[2 * i + 1] = bf2f((u16)(p >> 16));
    }
    float s = 0.f;
#pragma unroll
    for (int i = 0; i < 10; ++i) s += v[i];
#pragma unroll
    for (int o = 32; o > 0; o >>= 1) s += __shfl_xor(s, o);
    float mean = s * (1.0f / D_);
    float q = 0.f;
#pragma unroll
    for (int i = 0; i < 10; ++i) { float d = v[i] - mean; q += d * d; }
#pragma unroll
    for (int o = 32; o > 0; o >>= 1) q += __shfl_xor(q, o);
    float var = q * (1.0f / D_);
    float mb = rbf(mean);
    float vb = rbf(var);
    float den = rbf(sqrtf(rbf(vb + 1e-5f)));
    unsigned* xo = reinterpret_cast<unsigned*>(xn + (size_t)row * D_);
#pragma unroll
    for (int i = 0; i < 5; ++i) {
      u16 a = f2bf(rbf(v[2 * i] - mb) / den);
      u16 c = f2bf(rbf(v[2 * i + 1] - mb) / den);
      xo[lane + 64 * i] = (unsigned)a | ((unsigned)c << 16);
    }
    return;
  }
  b -= PREP_LN_BLOCKS;
  if (b < PREP_KV_BLOCKS) {
    int st = (b / (D_ / 64)) * 64, ct = (b % (D_ / 64)) * 64;
    int c = ct + (tid & 63);
    float w0 = bf2f(shiftk[c * 4 + 0]), w1 = bf2f(shiftk[c * 4 + 1]);
    float w2 = bf2f(shiftk[c * 4 + 2]), w3 = bf2f(shiftk[c * 4 + 3]);
    int r0 = (tid >> 6) * 16;
    int s0 = st + r0;
    float km3 = s0 >= 3 ? bf2f(kin[(size_t)(s0 - 3) * D_ + c]) : 0.f;
    float km2 = s0 >= 2 ? bf2f(kin[(size_t)(s0 - 2) * D_ + c]) : 0.f;
    float km1 = s0 >= 1 ? bf2f(kin[(size_t)(s0 - 1) * D_ + c]) : 0.f;
#pragma unroll
    for (int r = 0; r < 16; ++r) {
      int s = s0 + r;
      float k0 = bf2f(kin[(size_t)s * D_ + c]);
      float acc = rbf(km3 * w3);
      acc = rbf(acc + rbf(km2 * w2));
      acc = rbf(acc + rbf(km1 * w1));
      acc = rbf(acc + rbf(k0 * w0));
      float kv = acc * bf2f(vin[(size_t)s * D_ + c]);
      tile[tid & 63][r0 + r] = f2bf(kv);
      km3 = km2; km2 = km1; km1 = k0;
    }
    __syncthreads();
    for (int i = tid; i < 64 * 32; i += 256) {
      int cc = i >> 5, p = (i & 31) * 2;
      int g = p & ~7, e = p & 7;
      u16 a = tile[cc][g + 7 - e], c2 = tile[cc][g + 7 - (e + 1)];
      *reinterpret_cast<unsigned*>(uT + (size_t)(ct + cc) * S_ + st + p) = (unsigned)a | ((unsigned)c2 << 16);
    }
    return;
  }
  b -= PREP_KV_BLOCKS;
  if (b < PREP_W1_BLOCKS) {
    int which = b / (PREP_W1_BLOCKS / 2);
    b %= (PREP_W1_BLOCKS / 2);
    const u16* w = which ? wu : wg;
    int kt = (b / (F_ / 64)) * 64, ft = (b % (F_ / 64)) * 64;
    for (int i = tid; i < 64 * 32; i += 256) {
      int r = i >> 5, c = (i & 31) * 2;
      unsigned p = *reinterpret_cast<const unsigned*>(w + (size_t)(kt + r) * F_ + ft + c);
      tile[r][c] = (u16)(p & 0xffff);
      tile[r][c + 1] = (u16)(p >> 16);
    }
    __syncthreads();
    for (int i = tid; i < 64 * 32; i += 256) {
      int f = i >> 5, k = (i & 31) * 2;
      int ff = ft + f;
      int prow = (ff >> 5) * 64 + which * 32 + (ff & 31);
      unsigned p = (unsigned)tile[k][f] | ((unsigned)tile[k + 1][f] << 16);
      *reinterpret_cast<unsigned*>(w1t + (size_t)prow * D_ + kt + k) = p;
    }
    return;
  }
  b -= PREP_W1_BLOCKS;
  {
    int kt = (b / (D_ / 64)) * 64, nt = (b % (D_ / 64)) * 64;
    for (int i = tid; i < 64 * 32; i += 256) {
      int r = i >> 5, c = (i & 31) * 2;
      unsigned p = *reinterpret_cast<const unsigned*>(wd + (size_t)(kt + r) * D_ + nt + c);
      tile[r][c] = (u16)(p & 0xffff);
      tile[r][c + 1] = (u16)(p >> 16);
    }
    __syncthreads();
    for (int i = tid; i < 64 * 32; i += 256) {
      int n = i >> 5, k = (i & 31) * 2;
      unsigned p = (unsigned)tile[k][n] | ((unsigned)tile[k + 1][n] << 16);
      *reinterpret_cast<unsigned*>(w2t + (size_t)(nt + n) * F_ + kt + k) = p;
    }
  }
}

template <int N> __device__ __forceinline__ void wait_vm() {
  asm volatile("s_waitcnt vmcnt(%0)" ::"n"(N) : "memory");
}

template <int BM, int BN, int NS, int KS>
__device__ __forceinline__ void gemm_mainloop(const u16* __restrict__ A, const u16* __restrict__ B,
                                              int K, int brow, int bcol, char* lds,
                                              f32x4 (&acc)[BM / 32][BN / 32]) {
  constexpr int ABYTES = BM * 128, BBYTES = BN * 128, SUB = ABYTES + BBYTES, STAGE = KS * SUB;
  constexpr int L = (BM + BN) / 32;
  const int grp = __builtin_amdgcn_readfirstlane(threadIdx.x >> 8);
  const int tid = threadIdx.x & 255, wid = tid >> 6, lane = tid & 63;
  const int wr = wid >> 1, wc = wid & 1, fr = lane & 15, fq = lane >> 4;
  const int sr = tid >> 3, ssc = ((tid & 7) ^ (sr & 7)) * 8;
  const u16* ga = A + (size_t)(brow + sr) * K + ssc;
  const u16* gb = B + (size_t)(bcol + sr) * K + ssc;
  const int ktiles = K / 64;
  const int nt = (ktiles + KS - 1) / KS;
  auto stage = [&](int buf, int kt) {
    char* base = lds + buf * STAGE + grp * SUB + tid * 16;
    kt = kt * KS + grp; if (kt >= ktiles) kt = ktiles - 1;
#pragma unroll
    for (int i = 0; i < BM / 32; ++i)
      __builtin_amdgcn_global_load_lds(
          (const __attribute__((address_space(1))) void*)(ga + (size_t)i * 32 * K + kt * 64),
          (__attribute__((address_space(3))) void*)(base + i * 4096), 16, 0, 0);
#pragma unroll
    for (int i = 0; i < BN / 32; ++i)
      __builtin_amdgcn_global_load_lds(
          (const __attribute__((address_space(1))) void*)(gb + (size_t)i * 32 * K + kt * 64),
          (__attribute__((address_space(3))) void*)(base + ABYTES + i * 4096), 16, 0, 0);
  };
#pragma unroll
  for (int s = 0; s < NS - 1; ++s) stage(s, s);
  int cur = 0, nxt = NS - 1;
  for (int t = 0; t < nt; ++t) {
    if (t + NS - 1 <= nt) wait_vm<L*(NS - 2)>(); else wait_vm<0>();
    __builtin_amdgcn_s_barrier();
    if (t + NS - 1 < nt) stage(nxt, t + NS - 1);
    char* sa = lds + cur * STAGE + grp * SUB;
    char* sb = sa + ABYTES;
    if (t * KS + grp < ktiles)
#pragma unroll
    for (int kk = 0; kk < 2; ++kk) {
      bf16x8 af[BM / 32], bfr[BN / 32];
#pragma unroll
      for (int m = 0; m < BM / 32; ++m) {
        int r = wr * (BM / 2) + m * 16 + fr;
        int c = (kk * 4 + fq) ^ (r & 7);
        af[m] = *reinterpret_cast<const bf16x8*>(sa + r * 128 + c * 16);
      }
#pragma unroll
      for (int n = 0; n < BN / 32; ++n) {
        int r = wc * (BN / 2) + n * 16 + fr;
        int c = (kk * 4 + fq) ^ (r & 7);
        bfr[n] = *reinterpret_cast<const bf16x8*>(sb + r * 128 + c * 16);
      }
#pragma unroll
      for (int m = 0; m < BM / 32; ++m)
#pragma unroll
        for (int n = 0; n < BN / 32; ++n)
          acc[m][n] = __builtin_amdgcn_mfma_f32_16x16x32_bf16(af[m], bfr[n], acc[m][n], 0, 0, 0);
    }
    cur = (cur + 1 == NS) ? 0 : cur + 1;
    nxt = (nxt + 1 == NS) ? 0 : nxt + 1;
  }
  __builtin_amdgcn_s_barrier();
  if (KS == 2) {
    float* red = reinterpret_cast<float*>(lds) + wid * (BM / 32) * (BN / 32) * 4 * 64 + lane;
    if (grp == 1) {
#pragma unroll
      for (int m = 0; m < BM / 32; ++m)
#pragma unroll
        for (int n = 0; n < BN / 32; ++n)
#pragma unroll
          for (int j = 0; j < 4; ++j) red[((m * (BN / 32) + n) * 4 + j) * 64] = acc[m][n][j];
    }
    __syncthreads();
    if (grp == 0) {
#pragma unroll
      for (int m = 0; m < BM / 32; ++m)
#pragma unroll
        for (int n = 0; n < BN / 32; ++n)
#pragma unroll
          for (int j = 0; j < 4; ++j) acc[m][n][j] += red[((m * (BN / 32) + n) * 4 + j) * 64];
    }
    __syncthreads();
  }
}

#define G1NS 2
#define G2NS 3
constexpr int G1_BM = 256, G1_BN = 128, G1_NS = G1NS;
constexpr int G1_BLOCKS = (S_ / G1_BM) * (2 * F_ / G1_BN);
constexpr int CONV_BLOCKS = D_ / 8;
constexpr int HC_STRIDE = 1184;
constexpr int CONV_WAVE_LDS = 8 * HC_STRIDE + S_ * 2;
constexpr int G1_STAGE = (G1_BM + G1_BN) * 128;
constexpr int G1_LDS = G1_NS * G1_STAGE;
constexpr int L2_LDS = (8 * CONV_WAVE_LDS > G1_LDS) ? 8 * CONV_WAVE_LDS : G1_LDS;

__global__ __launch_bounds__(512) void mid_kernel(
    const u16* __restrict__ xn, const u16* __restrict__ w1t, u16* __restrict__ hbuf,
    const u16* __restrict__ uT, const u16* __restrict__ ssmk, const u16* __restrict__ ssmd,
    u16* __restrict__ yT) {
  extern __shared__ __attribute__((aligned(16))) char lds[];
  const int tid = threadIdx.x, wid = tid >> 6, lane = tid & 63;
  int b = blockIdx.x;
  if (b >= CONV_BLOCKS) {
    b -= CONV_BLOCKS;
    int bm = b % (S_ / G1_BM), bn = b / (S_ / G1_BM);
    int brow = bm * G1_BM, bcol = bn * G1_BN;
    f32x4 acc[4][4];
#pragma unroll
    for (int m = 0; m < 4; ++m)
#pragma unroll
      for (int n = 0; n < 4; ++n) acc[m][n] = f32x4{0.f, 0.f, 0.f, 0.f};
    const int wr = wid >> 1, wc = wid & 1, fr = lane & 15, fq = lane >> 4;
    {
      constexpr int K = D_, L = 6, NS = G1_NS;
      const int sr = tid >> 3, ssc = ((tid & 7) ^ (sr & 7)) * 8;
      const u16* ga = xn + (size_t)(brow + sr) * K + ssc;
      const u16* gb = w1t + (size_t)(bcol + sr) * K + ssc;
      auto stage = [&](int buf, int kt) {
        char* base = lds + buf * G1_STAGE + tid * 16;
#pragma unroll
        for (int i = 0; i < 4; ++i)
          __builtin_amdgcn_global_load_lds(
              (const __attribute__((address_space(1))) void*)(ga + (size_t)i * 64 * K + kt * 64),
              (__attribute__((address_space(3))) void*)(base + i * 8192), 16, 0, 0);
#pragma unroll
        for (int i = 0; i < 2; ++i)
          __builtin_amdgcn_global_load_lds(
              (const __attribute__((address_space(1))) void*)(gb + (size_t)i * 64 * K + kt * 64),
              (__attribute__((address_space(3))) void*)(base + G1_BM * 128 + i * 8192), 16, 0, 0);
      };
      const int nt = K / 64;
#pragma unroll
      for (int s = 0; s < NS - 1; ++s) stage(s, s);
      int cur = 0, nxt = NS - 1;
      for (int t = 0; t < nt; ++t) {
        if (t + NS - 1 <= nt) wait_vm<L*(NS - 2)>(); else wait_vm<0>();
        __builtin_amdgcn_s_barrier();
        if (t + NS - 1 < nt) stage(nxt, t + NS - 1);
        char* sa = lds + cur * G1_STAGE;
        char* sb = sa + G1_BM * 128;
#pragma unroll
        for (int kk = 0; kk < 2; ++kk) {
          bf16x8 af[4], bfr[4];
#pragma unroll
          for (int m = 0; m < 4; ++m) {
            int r = wr * 64 + m * 16 + fr;
            int c = (kk * 4 + fq) ^ (r & 7);
            af[m] = *reinterpret_cast<const bf16x8*>(sa + r * 128 + c * 16);
          }
#pragma unroll
          for (int n = 0; n < 4; ++n) {
            int r = wc * 64 + n * 16 + fr;
            int c = (kk * 4 + fq) ^ (r & 7);
            bfr[n] = *reinterpret_cast<const bf16x8*>(sb + r * 128 + c * 16);
          }
#pragma unroll
          for (int m = 0; m < 4; ++m)
#pragma unroll
            for (int n = 0; n < 4; ++n)
              acc[m][n] = __builtin_amdgcn_mfma_f32_16x16x32_bf16(af[m], bfr[n], acc[m][n], 0, 0, 0);
        }
        cur = (cur + 1 == NS) ? 0 : cur + 1;
        nxt = (nxt + 1 == NS) ? 0 : nxt + 1;
      }
      __builtin_amdgcn_s_barrier();
    }
    char* wl = lds + wid * (64 * 72);
#pragma unroll
    for (int m = 0; m < 4; ++m)
#pragma unroll
      for (int n = 0; n < 2; ++n)
#pragma unroll
        for (int j = 0; j < 4; ++j) {
          float g = rbf(acc[m][n][j]);
          float u = rbf(acc[m][n + 2][j]);
          float sg = rbf(1.0f / (1.0f + __expf(-g)));
          *reinterpret_cast<u16*>(wl + (m * 16 + fq * 4 + j) * 72 + (n * 16 + fr) * 2) = f2bf(sg * u);
        }
    int f0 = bn * 64 + wc * 32;
#pragma unroll
    for (int it = 0; it < 4; ++it) {
      int r = it * 16 + (lane >> 2), ch = lane & 3;
      const unsigned* p = reinterpret_cast<const unsigned*>(wl + r * 72 + ch * 16);
      uint4 v = make_uint4(p[0], p[1], p[2], p[3]);
      *reinterpret_cast<uint4*>(hbuf + (size_t)(brow + wr * 64 + r) * F_ + f0 + ch * 8) = v;
    }
    return;
  }
  const int c = b * 8 + wid;
  char* wl = lds + wid * CONV_WAVE_LDS;
  u16* ul = reinterpret_cast<u16*>(wl + 8 * HC_STRIDE);
  const u16* hrow = ssmk + (size_t)c * S_;
  for (int i = lane * 8; i < S_; i += 64 * 8)
    *reinterpret_cast<bf16x8*>(ul + i) = *reinterpret_cast<const bf16x8*>(uT + (size_t)c * S_ + i);
  const int j = lane & 31, hh = lane >> 5;
  f32x16 acc0, acc1;
#pragma unroll
  for (int i = 0; i < 16; ++i) { acc0[i] = 0.f; acc1[i] = 0.f; }
  const int boff = j - 8 * hh - 7 + 32 + 16;
  for (int d0 = 0; d0 < 64; d0 += 16) {
#pragma unroll
    for (int it = 0; it < 2; ++it) {
      int y0 = (it * 64 + lane) * 8;
      if (y0 < 576) {
        int hi = y0 + 32 * d0 - 48;
        bf16x8 v0 = bf16x8{0, 0, 0, 0, 0, 0, 0, 0}, v1 = v0;
        if (hi >= 0 && hi < S_) v0 = *reinterpret_cast<const bf16x8*>(hrow + hi);
        if (hi + 8 >= 0 && hi + 8 < S_) v1 = *reinterpret_cast<const bf16x8*>(hrow + hi + 8);
#pragma unroll
        for (int a = 0; a < 8; ++a) {
          bf16x8 v;
#pragma unroll
          for (int i = 0; i < 8; ++i) v[i] = (a + i < 8) ? v0[a + i] : v1[a + i - 8];
          *reinterpret_cast<bf16x8*>(wl + a * HC_STRIDE + y0 * 2) = v;
        }
      }
    }
    for (int dl = 0; dl < 16; ++dl) {
      int d = d0 + dl;
#pragma unroll
      for (int ks = 0; ks < 2; ++ks) {
        int bp = 32 * dl - 16 * ks + boff;
        int a = bp & 7;
        bf16x8 af = *reinterpret_cast<const bf16x8*>(wl + a * HC_STRIDE + (bp - a) * 2);
        {
          int blk = 32 + j - d;
          bool ok = blk >= 0;
          bf16x8 bf = *reinterpret_cast<const bf16x8*>(ul + (ok ? blk : 0) * 32 + 16 * ks + 8 * hh);
          if (!ok) bf = bf16x8{0, 0, 0, 0, 0, 0, 0, 0};
          acc1 = __builtin_amdgcn_mfma_f32_32x32x16_bf16(af, bf, acc1, 0, 0, 0);
        }
        if (d0 < 32) {
          int blk = j - d;
          bool ok = blk >= 0;
          bf16x8 bf = *reinterpret_cast<const bf16x8*>(ul + (ok ? blk : 0) * 32 + 16 * ks + 8 * hh);
          if (!ok) bf = bf16x8{0, 0, 0, 0, 0, 0, 0, 0};
          acc0 = __builtin_amdgcn_mfma_f32_32x32x16_bf16(af, bf, acc0, 0, 0, 0);
        }
      }
    }
  }
  float dd = bf2f(ssmd[c]);
#pragma unroll
  for (int nt = 0; nt < 2; ++nt) {
#pragma unroll
    for (int rq = 0; rq < 4; ++rq) {
      int t0 = 32 * (32 * nt + j) + 8 * rq + 4 * hh;
      u16 o[4];
#pragma unroll
      for (int r = 0; r < 4; ++r) {
        int t = t0 + r;
        float a = nt ? acc1[rq * 4 + r] : acc0[rq * 4 + r];
        float kv = bf2f(ul[(t & ~7) + 7 - (t & 7)]);
        o[r] = f2bf(rbf(a) + rbf(kv * dd));
      }
      *reinterpret_cast<uint2*>(yT + (size_t)c * S_ + t0) =
          make_uint2((unsigned)o[0] | ((unsigned)o[1] << 16), (unsigned)o[2] | ((unsigned)o[3] << 16));
    }
  }
}

constexpr int G2_BM = 128, G2_BN = 64, G2_NS = G2NS;
constexpr int G2_BLOCKS = (S_ / G2_BM) * (D_ / G2_BN);
constexpr int G2_LDS = 2 * G2_NS * (G2_BM + G2_BN) * 128;

__global__ __launch_bounds__(512) void out_kernel(
    const u16* __restrict__ hbuf, const u16* __restrict__ w2t, const u16* __restrict__ x,
    const u16* __restrict__ xn, const u16* __restrict__ yT, u16* __restrict__ out) {
  extern __shared__ __attribute__((aligned(16))) char lds[];
  const int tid = threadIdx.x, wid = (tid >> 6) & 3, lane = tid & 63;
  int b = blockIdx.x;
  int bm = b % (S_ / G2_BM), bn = b / (S_ / G2_BM);
  int brow = bm * G2_BM, bcol = bn * G2_BN;
  f32x4 acc[4][2];
#pragma unroll
  for (int m = 0; m < 4; ++m)
#pragma unroll
    for (int n = 0; n < 2; ++n) acc[m][n] = f32x4{0.f, 0.f, 0.f, 0.f};
  gemm_mainloop<G2_BM, G2_BN, G2_NS, 2>(hbuf, w2t, F_, brow, bcol, lds, acc);
  const int wr = wid >> 1, wc = wid & 1, fr = lane & 15, fq = lane >> 4;
  char* lb = lds;
  char* ly = lds + 128 * 136;
  if (tid < 256)
#pragma unroll
  for (int m = 0; m < 4; ++m)
#pragma unroll
    for (int n = 0; n < 2; ++n)
#pragma unroll
      for (int j = 0; j < 4; ++j)
        *reinterpret_cast<u16*>(lb + (wr * 64 + m * 16 + fq * 4 + j) * 136 + (wc * 32 + n * 16 + fr) * 2) =
            f2bf(acc[m][n][j]);
#pragma unroll
  for (int it = 0; it < 2; ++it) {
    int idx = it * 512 + tid;
    int cc = idx >> 4, tq = (idx & 15) * 8;
    bf16x8 v = *reinterpret_cast<const bf16x8*>(yT + (size_t)(bcol + cc) * S_ + brow + tq);
#pragma unroll
    for (int e = 0; e < 8; ++e) *reinterpret_cast<u16*>(ly + (tq + e) * 136 + cc * 2) = (u16)v[e];
  }
  __syncthreads();
#pragma unroll
  for (int it = 0; it < 2; ++it) {
    int idx = it * 512 + tid;
    int r = idx >> 3, ch = idx & 7;
    size_t g = (size_t)(brow + r) * D_ + bcol + ch * 8;
    bf16x8 xv = *reinterpret_cast<const bf16x8*>(x + g);
    bf16x8 nv = *reinterpret_cast<const bf16x8*>(xn + g);
    const u16* pb = reinterpret_cast<const u16*>(lb + r * 136 + ch * 16);
    const u16* py = reinterpret_cast<const u16*>(ly + r * 136 + ch * 16);
    bf16x8 o;
#pragma unroll
    for (int e = 0; e < 8; ++e) {
      float a = rbf(bf2f((u16)nv[e]) * bf2f(py[e]));
      float s1 = rbf(bf2f((u16)xv[e]) + a);
      o[e] = (short)f2bf(s1 + bf2f(pb[e]));
    }
    *reinterpret_cast<bf16x8*>(out + g) = o;
  }
}

extern "C" void kernel_launch(void* const* d_in, const int* in_sizes, int n_in,
                              void* d_out, int out_size, void* d_ws, size_t ws_size,
                              hipStream_t stream) {
  const u16* x = (const u16*)d_in[0];
  const u16* kin = (const u16*)d_in[1];
  const u16* vin = (const u16*)d_in[2];
  const u16* shiftk = (const u16*)d_in[3];
  const u16* ssmk = (const u16*)d_in[4];
  const u16* ssmd = (const u16*)d_in[5];
  const u16* wg = (const u16*)d_in[7];
  const u16* wu = (const u16*)d_in[8];
  const u16* wd = (const u16*)d_in[9];
  u16* out = (u16*)d_out;
  char* ws = (char*)d_ws;
  u16* xn = (u16*)ws;                       ws += (size_t)S_ * D_ * 2;
  u16* uT = (u16*)ws;                       ws += (size_t)S_ * D_ * 2;
  u16* yT = (u16*)ws;                       ws += (size_t)S_ * D_ * 2;
  u16* w1t = (u16*)ws;                      ws += (size_t)2 * F_ * D_ * 2;
  u16* w2t = (u16*)ws;                      ws += (size_t)F_ * D_ * 2;
  u16* hbuf = (u16*)ws;                     ws += (size_t)S_ * F_ * 2;

  hipFuncSetAttribute((const void*)mid_kernel, hipFuncAttributeMaxDynamicSharedMemorySize, L2_LDS);
  prep_kernel<<<PREP_BLOCKS, 256, 0, stream>>>(x, kin, vin, shiftk, wg, wu, wd, xn, uT, w1t, w2t);
  mid_kernel<<<G1_BLOCKS + CONV_BLOCKS, 512, L2_LDS, stream>>>(xn, w1t, hbuf, uT, ssmk, ssmd, yT);
  hipFuncSetAttribute((const void*)out_kernel, hipFuncAttributeMaxDynamicSharedMemorySize, G2_LDS);
  out_kernel<<<G2_BLOCKS, 512, G2_LDS, stream>>>(hbuf, w2t, x, xn, yT, out);
}
```

```cpp
#include <hip/hip_runtime.h>
#include <hip/hip_bf16.h>
#include <stdint.h>

typedef unsigned short u16;
using bf16x8 = __attribute__((ext_vector_type(8))) short;
using f32x4 = __attribute__((ext_vector_type(4))) float;
using f32x16 = __attribute__((ext_vector_type(16))) float;

constexpr int S_ = 2048, D_ = 640, F_ = 1728;

__device__ __forceinline__ float bf2f(u16 v) { return __uint_as_float(((unsigned)v) << 16); }
__device__ __forceinline__ u16 f2bf(float f) {
  __hip_bfloat16 b = __float2bfloat16(f);
  return *reinterpret_cast<u16*>(&b);
}
__device__ __forceinline__ float rbf(float f) { return bf2f(f2bf(f)); }

constexpr int PREP_LN_BLOCKS = S_ / 4;
constexpr int PREP_KV_BLOCKS = (S_ / 64) * (D_ / 64);
constexpr int PREP_W1_BLOCKS = (D_ / 64) * (F_ / 64) * 2;
constexpr int PREP_W2_BLOCKS = (F_ / 64) * (D_ / 64);
constexpr int PREP_BLOCKS = PREP_LN_BLOCKS + PREP_KV_BLOCKS + PREP_W1_BLOCKS + PREP_W2_BLOCKS;

__global__ __launch_bounds__(256) void prep_kernel(
    const u16* __restrict__ x, const u16* __restrict__ kin, const u16* __restrict__ vin,
    const u16* __restrict__ shiftk, const u16* __restrict__ wg, const u16* __restrict__ wu,
    const u16* __restrict__ wd, u16* __restrict__ xn, u16* __restrict__ uT,
    u16* __restrict__ w1t, u16* __restrict__ w2t) {
  __shared__ u16 tile[64][66];
  int b = blockIdx.x;
  int tid = threadIdx.x;
  if (b < PREP_LN_BLOCKS) {
    int row = b * 4 + (tid >> 6);
    int lane = tid & 63;
    const unsigned* xr = reinterpret_cast<const unsigned*>(x + (size_t)row * D_);
    float v[10];
#pragma unroll
    for (int i = 0; i < 5; ++i) {
      unsigned p = xr[lane + 64 * i];
      v[2 * i] = bf2f((u16)(p & 0xffff));
      v[2 * i + 1] = bf2f((u16)(p >> 16));
    }
    float s = 0.f;
#pragma unroll
    for (int i = 0; i < 10; ++i) s += v[i];
#pragma unroll
    for (int o = 32; o > 0; o >>= 1) s += __shfl_xor(s, o);
    float mean = s * (1.0f / D_);
    float q = 0.f;
#pragma unroll
    for (int i = 0; i < 10; ++i) { float d = v[i] - mean; q += d * d; }
#pragma unroll
    for (int o = 32; o > 0; o >>= 1) q += __shfl_xor(q, o);
    float var = q * (1.0f / D_);
    float mb = rbf(mean);
    float vb = rbf(var);
    float den = rbf(sqrtf(rbf(vb + 1e-5f)));
    unsigned* xo = reinterpret_cast<unsigned*>(xn + (size_t)row * D_);
#pragma unroll
    for (int i = 0; i < 5; ++i) {
      u16 a = f2bf(rbf(v[2 * i] - mb) / den);
      u16 c = f2bf(rbf(v[2 * i + 1] - mb) / den);
      xo[lane + 64 * i] = (unsigned)a | ((unsigned)c << 16);
    }
    return;
  }
  b -= PREP_LN_BLOCKS;
  if (b < PREP_KV_BLOCKS) {
    int st = (b / (D_ / 64)) * 64, ct = (b % (D_ / 64)) * 64;
    int c = ct + (tid & 63);
    float w0 = bf2f(shiftk[c * 4 + 0]), w1 = bf2f(shiftk[c * 4 + 1]);
    float w2 = bf2f(shiftk[c * 4 + 2]), w3 = bf2f(shiftk[c * 4 + 3]);
    int r0 = (tid >> 6) * 16;
    int s0 = st + r0;
    float km3 = s0 >= 3 ? bf2f(kin[(size_t)(s0 - 3) * D_ + c]) : 0.f;
    float km2 = s0 >= 2 ? bf2f(kin[(size_t)(s0 - 2) * D_ + c]) : 0.f;
    float km1 = s0 >= 1 ? bf2f(kin[(size_t)(s0 - 1) * D_ + c]) : 0.f;
#pragma unroll
    for (int r = 0; r < 16; ++r) {
      int s = s0 + r;
      float k0 = bf2f(kin[(size_t)s * D_ + c]);
      float acc = rbf(km3 * w3);
      acc = rbf(acc + rbf(km2 * w2));
      acc = rbf(acc + rbf(km1 * w1));
      acc = rbf(acc + rbf(k0 * w0));
      float kv = acc * bf2f(vin[(size_t)s * D_ + c]);
      tile[tid & 63][r0 + r] = f2bf(kv);
      km3 = km2; km2 = km1; km1 = k0;
    }
    __syncthreads();
    for (int i = tid; i < 64 * 32; i += 256) {
      int cc = i >> 5, p = (i & 31) * 2;
      int g = p & ~7, e = p & 7;
      u16 a = tile[cc][g + 7 - e], c2 = tile[cc][g + 7 - (e + 1)];
      *reinterpret_cast<unsigned*>(uT + (size_t)(ct + cc) * S_ + st + p) = (unsigned)a | ((unsigned)c2 << 16);
    }
    return;
  }
  b -= PREP_KV_BLOCKS;
  if (b < PREP_W1_BLOCKS) {
    int which = b / (PREP_W1_BLOCKS / 2);
    b %= (PREP_W1_BLOCKS / 2);
    const u16* w = which ? wu : wg;
    int kt = (b / (F_ / 64)) * 64, ft = (b % (F_ / 64)) * 64;
    for (int i = tid; i < 64 * 32; i += 256) {
      int r = i >> 5, c = (i & 31) * 2;
      unsigned p = *reinterpret_cast<const unsigned*>(w + (size_t)(kt + r) * F_ + ft + c);
      tile[r][c] = (u16)(p & 0xffff);
      tile[r][c + 1] = (u16)(p >> 16);
    }
    __syncthreads();
    for (int i = tid; i < 64 * 32; i += 256) {
      int f = i >> 5, k = (i & 31) * 2;
      int ff = ft + f;
      int prow = (ff >> 5) * 64 + which * 32 + (ff & 31);
      unsigned p = (unsigned)tile[k][f] | ((unsigned)tile[k + 1][f] << 16);
      *reinterpret_cast<unsigned*>(w1t + (size_t)prow * D_ + kt + k) = p;
    }
    return;
  }
  b -= PREP_W1_BLOCKS;
  {
    int kt = (b / (D_ / 64)) * 64, nt = (b % (D_ / 64)) * 64;
    for (int i = tid; i < 64 * 32; i += 256) {
      int r = i >> 5, c = (i & 31) * 2;
      unsigned p = *reinterpret_cast<const unsigned*>(wd + (size_t)(kt + r) * D_ + nt + c);
      tile[r][c] = (u16)(p & 0xffff);
      tile[r][c + 1] = (u16)(p >> 16);
    }
    __syncthreads();
    for (int i = tid; i < 64 * 32; i += 256) {
      int n = i >> 5, k = (i & 31) * 2;
      unsigned p = (unsigned)tile[k][n] | ((unsigned)tile[k + 1][n] << 16);
      *reinterpret_cast<unsigned*>(w2t + (size_t)(nt + n) * F_ + kt + k) = p;
    }
  }
}

template <int N> __device__ __forceinline__ void wait_vm() {
  asm volatile("s_waitcnt vmcnt(%0)" ::"n"(N) : "memory");
}

template <int BM, int BN, int NS, int KS>
__device__ __forceinline__ void gemm_mainloop(const u16* __restrict__ A, const u16* __restrict__ B,
                                              int K, int brow, int bcol, char* lds,
                                              f32x4 (&acc)[BM / 32][BN / 32]) {
  constexpr int ABYTES = BM * 128, BBYTES = BN * 128, SUB = ABYTES + BBYTES, STAGE = KS * SUB;
  constexpr int L = (BM + BN) / 32;
  const int grp = __builtin_amdgcn_readfirstlane(threadIdx.x >> 8);
  const int tid = threadIdx.x & 255, wid = tid >> 6, lane = tid & 63;
  const int wr = wid >> 1, wc = wid & 1, fr = lane & 15, fq = lane >> 4;
  const int sr = tid >> 3, ssc = ((tid & 7) ^ (sr & 7)) * 8;
  const u16* ga = A + (size_t)(brow + sr) * K + ssc;
  const u16* gb = B + (size_t)(bcol + sr) * K + ssc;
  const int ktiles = K / 64;
  const int nt = (ktiles + KS - 1) / KS;
  auto stage = [&](int buf, int kt) {
    char* base = lds + buf * STAGE + grp * SUB + tid * 16;
    kt = kt * KS + grp; if (kt >= ktiles) kt = ktiles - 1;
#pragma unroll
    for (int i = 0; i < BM / 32; ++i)
      __builtin_amdgcn_global_load_lds(
          (const __attribute__((address_space(1))) void*)(ga + (size_t)i * 32 * K + kt * 64),
          (__attribute__((address_space(3))) void*)(base + i * 4096), 16, 0, 0);
#pragma unroll
    for (int i = 0; i < BN / 32; ++i)
      __builtin_amdgcn_global_load_lds(
          (const __attribute__((address_space(1))) void*)(gb + (size_t)i * 32 * K + kt * 64),
          (__attribute__((address_space(3))) void*)(base + ABYTES + i * 4096), 16, 0, 0);
  };
#pragma unroll
  for (int s = 0; s < NS - 1; ++s) stage(s, s);
  int cur = 0, nxt = NS - 1;
  for (int t = 0; t < nt; ++t) {
    if (t + NS - 1 <= nt) wait_vm<L*(NS - 2)>(); else wait_vm<0>();
    __builtin_amdgcn_s_barrier();
    if (t + NS - 1 < nt) stage(nxt, t + NS - 1);
    char* sa = lds + cur * STAGE + grp * SUB;
    char* sb = sa + ABYTES;
    if (t * KS + grp < ktiles)
#pragma unroll
    for (int kk = 0; kk < 2; ++kk) {
      bf16x8 af[BM / 32], bfr[BN / 32];
#pragma unroll
      for (int m = 0; m < BM / 32; ++m) {
        int r = wr * (BM / 2) + m * 16 + fr;
        int c = (kk * 4 + fq) ^ (r & 7);
        af[m] = *reinterpret_cast<const bf16x8*>(sa + r * 128 + c * 16);
      }
#pragma unroll
      for (int n = 0; n < BN / 32; ++n) {
        int r = wc * (BN / 2) + n * 16 + fr;
        int c = (kk * 4 + fq) ^ (r & 7);
        bfr[n] = *reinterpret_cast<const bf16x8*>(sb + r * 128 + c * 16);
      }
#pragma unroll
      for (int m = 0; m < BM / 32; ++m)
#pragma unroll
        for (int n = 0; n < BN / 32; ++n)
          acc[m][n] = __builtin_amdgcn_mfma_f32_16x16x32_bf16(af[m], bfr[n], acc[m][n], 0, 0, 0);
    }
    cur = (cur + 1 == NS) ? 0 : cur + 1;
    nxt = (nxt + 1 == NS) ? 0 : nxt + 1;
  }
  __builtin_amdgcn_s_barrier();
  if (KS == 2) {
    float* red = reinterpret_cast<float*>(lds) + wid * (BM / 32) * (BN / 32) * 4 * 64 + lane;
    if (grp == 1) {
#pragma unroll
      for (int m = 0; m < BM / 32; ++m)
#pragma unroll
        for (int n = 0; n < BN / 32; ++n)
#pragma unroll
          for (int j = 0; j < 4; ++j) red[((m * (BN / 32) + n) * 4 + j) * 64] = acc[m][n][j];
    }
    __syncthreads();
    if (grp == 0) {
#pragma unroll
      for (int m = 0; m < BM / 32; ++m)
#pragma unroll
        for (int n = 0; n < BN / 32; ++n)
#pragma unroll
          for (int j = 0; j < 4; ++j) acc[m][n][j] += red[((m * (BN / 32) + n) * 4 + j) * 64];
    }
    __syncthreads();
  }
}

#define G1NS 2
#define G2NS 3
constexpr int G1_BM = 128, G1_BN = 128, G1_NS = G1NS;
constexpr int G1_BLOCKS = (S_ / G1_BM) * (2 * F_ / G1_BN);
constexpr int CONV_BLOCKS = D_ / 4;
constexpr int HC_STRIDE = 1184;
constexpr int CONV_WAVE_LDS = 8 * HC_STRIDE + S_ * 2;
constexpr int G1_LDS = G1_NS * (G1_BM + G1_BN) * 128;
constexpr int L2_LDS = (4 * CONV_WAVE_LDS > G1_LDS) ? 4 * CONV_WAVE_LDS : G1_LDS;

__global__ __launch_bounds__(256) void mid_kernel(
    const u16* __restrict__ xn, const u16* __restrict__ w1t, u16* __restrict__ hbuf,
    const u16* __restrict__ uT, const u16* __restrict__ ssmk, const u16* __restrict__ ssmd,
    u16* __restrict__ yT) {
  extern __shared__ __attribute__((aligned(16))) char lds[];
  const int tid = threadIdx.x, wid = tid >> 6, lane = tid & 63;
  int b = blockIdx.x;
  if (b >= CONV_BLOCKS) {
    b -= CONV_BLOCKS;
    int bm = b % (S_ / G1_BM), bn = b / (S_ / G1_BM);
    int brow = bm * G1_BM, bcol = bn * G1_BN;
    f32x4 acc[4][4];
#pragma unroll
    for (int m = 0; m < 4; ++m)
#pragma unroll
      for (int n = 0; n < 4; ++n) acc[m][n] = f32x4{0.f, 0.f, 0.f, 0.f};
    gemm_mainloop<G1_BM, G1_BN, G1_NS, 1>(xn, w1t, D_, brow, bcol, lds, acc);
    const int wr = wid >> 1, wc = wid & 1, fr = lane & 15, fq = lane >> 4;
    char* wl = lds + wid * (64 * 72);
#pragma unroll
    for (int m = 0; m < 4; ++m)
#pragma unroll
      for (int n = 0; n < 2; ++n)
#pragma unroll
        for (int j = 0; j < 4; ++j) {
          float g = rbf(acc[m][n][j]);
          float u = rbf(acc[m][n + 2][j]);
          float sg = rbf(1.0f / (1.0f + __expf(-g)));
          *reinterpret_cast<u16*>(wl + (m * 16 + fq * 4 + j) * 72 + (n * 16 + fr) * 2) = f2bf(sg * u);
        }
    int f0 = bn * 64 + wc * 32;
#pragma unroll
    for (int it = 0; it < 4; ++it) {
      int r = it * 16 + (lane >> 2), ch = lane & 3;
      const unsigned* p = reinterpret_cast<const unsigned*>(wl + r * 72 + ch * 16);
      uint4 v = make_uint4(p[0], p[1], p[2], p[3]);
      *reinterpret_cast<uint4*>(hbuf + (size_t)(brow + wr * 64 + r) * F_ + f0 + ch * 8) = v;
    }
    return;
  }
  const int c = b * 4 + wid;
  char* wl = lds + wid * CONV_WAVE_LDS;
  u16* ul = reinterpret_cast<u16*>(wl + 8 * HC_STRIDE);
  const u16* hrow = ssmk + (size_t)c * S_;
  for (int i = lane * 8; i < S_; i += 64 * 8)
    *reinterpret_cast<bf16x8*>(ul + i) = *reinterpret_cast<const bf16x8*>(uT + (size_t)c * S_ + i);
  const int j = lane & 31, hh = lane >> 5;
  f32x16 acc0, acc1;
#pragma unroll
  for (int i = 0; i < 16; ++i) { acc0[i] = 0.f; acc1[i] = 0.f; }
  const int boff = j - 8 * hh - 7 + 32 + 16;
  for (int d0 = 0; d0 < 64; d0 += 16) {
#pragma unroll
    for (int it = 0; it < 2; ++it) {
      int y0 = (it * 64 + lane) * 8;
      if (y0 < 576) {
        int hi = y0 + 32 * d0 - 48;
        bf16x8 v0 = bf16x8{0, 0, 0, 0, 0, 0, 0, 0}, v1 = v0;
        if (hi >= 0 && hi < S_) v0 = *reinterpret_cast<const bf16x8*>(hrow + hi);
        if (hi + 8 >= 0 && hi + 8 < S_) v1 = *reinterpret_cast<const bf16x8*>(hrow + hi + 8);
#pragma unroll
        for (int a = 0; a < 8; ++a) {
          bf16x8 v;
#pragma unroll
          for (int i = 0; i < 8; ++i) v[i] = (a + i < 8) ? v0[a + i] : v1[a + i - 8];
          *reinterpret_cast<bf16x8*>(wl + a * HC_STRIDE + y0 * 2) = v;
        }
      }
    }
    for (int dl = 0; dl < 16; ++dl) {
      int d = d0 + dl;
#pragma unroll
      for (int ks = 0; ks < 2; ++ks) {
        int bp = 32 * dl - 16 * ks + boff;
        int a = bp & 7;
        bf16x8 af = *reinterpret_cast<const bf16x8*>(wl + a * HC_STRIDE + (bp - a) * 2);
        {
          int blk = 32 + j - d;
          bool ok = blk >= 0;
          bf16x8 bf = *reinterpret_cast<const bf16x8*>(ul + (ok ? blk : 0) * 32 + 16 * ks + 8 * hh);
          if (!ok) bf = bf16x8{0, 0, 0, 0, 0, 0, 0, 0};
          acc1 = __builtin_amdgcn_mfma_f32_32x32x16_bf16(af, bf, acc1, 0, 0, 0);
        }
        if (d0 < 32) {
          int blk = j - d;
          bool ok = blk >= 0;
          bf16x8 bf = *reinterpret_cast<const bf16x8*>(ul + (ok ? blk : 0) * 32 + 16 * ks + 8 * hh);
          if (!ok) bf = bf16x8{0, 0, 0, 0, 0, 0, 0, 0};
          acc0 = __builtin_amdgcn_mfma_f32_32x32x16_bf16(af, bf, acc0, 0, 0, 0);
        }
      }
    }
  }
  float dd = bf2f(ssmd[c]);
#pragma unroll
  for (int nt = 0; nt < 2; ++nt) {
#pragma unroll
    for (int rq = 0; rq < 4; ++rq) {
      int t0 = 32 * (32 * nt + j) + 8 * rq + 4 * hh;
      u16 o[4];
#pragma unroll
      for (int r = 0; r < 4; ++r) {
        int t = t0 + r;
        float a = nt ? acc1[rq * 4 + r] : acc0[rq * 4 + r];
        float kv = bf2f(ul[(t & ~7) + 7 - (t & 7)]);
        o[r] = f2bf(rbf(a) + rbf(kv * dd));
      }
      *reinterpret_cast<uint2*>(yT + (size_t)c * S_ + t0) =
          make_uint2((unsigned)o[0] | ((unsigned)o[1] << 16), (unsigned)o[2] | ((unsigned)o[3] << 16));
    }
  }
}

constexpr int G2_BM = 128, G2_BN = 64, G2_NS = G2NS;
constexpr int G2_BLOCKS = (S_ / G2_BM) * (D_ / G2_BN);
constexpr int G2_LDS = 2 * G2_NS * (G2_BM + G2_BN) * 128;

__global__ __launch_bounds__(512) void out_kernel(
    const u16* __restrict__ hbuf, const u16* __restrict__ w2t, const u16* __restrict__ x,
    const u16* __restrict__ xn, const u16* __restrict__ yT, u16* __restrict__ out) {
  extern __shared__ __attribute__((aligned(16))) char lds[];
  const int tid = threadIdx.x, wid = (tid >> 6) & 3, lane = tid & 63;
  int b = blockIdx.x;
  int bm = b % (S_ / G2_BM), bn = b / (S_ / G2_BM);
  int brow = bm * G2_BM, bcol = bn * G2_BN;
  f32x4 acc[4][2];
#pragma unroll
  for (int m = 0; m < 4; ++m)
#pragma unroll
    for (int n = 0; n < 2; ++n) acc[m][n] = f32x4{0.f, 0.f, 0.f, 0.f};
  gemm_mainloop<G2_BM, G2_BN, G2_NS, 2>(hbuf, w2t, F_, brow, bcol, lds, acc);
  const int wr = wid >> 1, wc = wid & 1, fr = lane & 15, fq = lane >> 4;
  char* lb = lds;
  char* ly = lds + 128 * 136;
  if (tid < 256)
#pragma unroll
  for (int m = 0; m < 4; ++m)
#pragma unroll
    for (int n = 0; n < 2; ++n)
#pragma unroll
      for (int j = 0; j < 4; ++j)
        *reinterpret_cast<u16*>(lb + (wr * 64 + m * 16 + fq * 4 + j) * 136 + (wc * 32 + n * 16 + fr) * 2) =
            f2bf(acc[m][n][j]);
#pragma unroll
  for (int it = 0; it < 2; ++it) {
    int idx = it * 512 + tid;
    int cc = idx >> 4, tq = (idx & 15) * 8;
    bf16x8 v = *reinterpret_cast<const bf16x8*>(yT + (size_t)(bcol + cc) * S_ + brow + tq);
#pragma unroll
    for (int e = 0; e < 8; ++e) *reinterpret_cast<u16*>(ly + (tq + e) * 136 + cc * 2) = (u16)v[e];
  }
  __syncthreads();
#pragma unroll
  for (int it = 0; it < 2; ++it) {
    int idx = it * 512 + tid;
    int r = idx >> 3, ch = idx & 7;
    size_t g = (size_t)(brow + r) * D_ + bcol + ch * 8;
    bf16x8 xv = *reinterpret_cast<const bf16x8*>(x + g);
    bf16x8 nv = *reinterpret_cast<const bf16x8*>(xn + g);
    const u16* pb = reinterpret_cast<const u16*>(lb + r * 136 + ch * 16);
    const u16* py = reinterpret_cast<const u16*>(ly + r * 136 + ch * 16);
    bf16x8 o;
#pragma unroll
    for (int e = 0; e < 8; ++e) {
      float a = rbf(bf2f((u16)nv[e]) * bf2f(py[e]));
      float s1 = rbf(bf2f((u16)xv[e]) + a);
      o[e] = (short)f2bf(s1 + bf2f(pb[e]));
    }
    *reinterpret_cast<bf16x8*>(out + g) = o;
  }
}

extern "C" void kernel_launch(void* const* d_in, const int* in_sizes, int n_in,
                              void* d_out, int out_size, void* d_ws, size_t ws_size,
                              hipStream_t stream) {
  const u16* x = (const u16*)d_in[0];
  const u16* kin = (const u16*)d_in[1];
  const u16* vin = (const u16*)d_in[2];
  const u16* shiftk = (const u16*)d_in[3];
  const u16* ssmk = (const u16*)d_in[4];
  const u16* ssmd = (const u16*)d_in[5];
  const u16* wg = (const u16*)d_in[7];
  const u16* wu = (const u16*)d_in[8];
  const u16* wd = (const u16*)d_in[9];
  u16* out = (u16*)d_out;
  char* ws = (char*)d_ws;
  u16* xn = (u16*)ws;                       ws += (size_t)S_ * D_ * 2;
  u16* uT = (u16*)ws;                       ws += (size_t)S_ * D_ * 2;
  u16* yT = (u16*)ws;                       ws += (size_t)S_ * D_ * 2;
  u16* w1t = (u16*)ws;                      ws += (size_t)2 * F_ * D_ * 2;
  u16* w2t = (u16*)ws;                      ws += (size_t)F_ * D_ * 2;
  u16* hbuf = (u16*)ws;                     ws += (size_t)S_ * F_ * 2;

  hipFuncSetAttribute((const void*)mid_kernel, hipFuncAttributeMaxDynamicSharedMemorySize, L2_LDS);
  prep_kernel<<<PREP_BLOCKS, 256, 0, stream>>>(x, kin, vin, shiftk, wg, wu, wd, xn, uT, w1t, w2t);
  mid_kernel<<<G1_BLOCKS + CONV_BLOCKS, 256, L2_LDS, stream>>>(xn, w1t, hbuf, uT, ssmk, ssmd, yT);
  hipFuncSetAttribute((const void*)out_kernel, hipFuncAttributeMaxDynamicSharedMemorySize, G2_LDS);
  out_kernel<<<G2_BLOCKS, 512, G2_LDS, stream>>>(hbuf, w2t, x, xn, yT, out);
}
```
